```python
import torch

def _softmax(x, axis=-1):
    return torch.softmax(x, dim=axis)
def _gelu(x):
    return torch.nn.functional.gelu(x, approximate="tanh")
def _silu(x):
    return torch.nn.functional.silu(x)
def _sigmoid(x):
    return torch.sigmoid(x)
def _layernorm(x, axis=-1, eps=1e-5):
    m = x.mean(dim=axis, keepdim=True)
    v = x.var(dim=axis, keepdim=True, unbiased=False)
    return (x - m) / torch.sqrt(v + eps)
def _scatter_add(tgt, idx, src, axis=0):
    return tgt.clone().index_add_(axis, idx, src)
def _segment_sum(src, seg, n):
    out = torch.zeros((n,) + src.shape[1:], dtype=src.dtype)
    return out.index_add_(0, seg, src)
def _pad(x, pw):
    flat = tuple(v for pair in reversed(pw) for v in pair)
    return torch.nn.functional.pad(x, flat)

kernel_name = 'recurrent_scan__sliding_block_cached__geglu_ffn__decode'

def setup_inputs():

    _w = (lambda a: (lambda t: t.to(torch.bfloat16) if t.is_floating_point() else t)(torch.as_tensor(a)))
    def _A_setup():
        B, Sq, D = 1, 1, 640
        Hq, Hkv, Sk, Dh = 32, 32, 32768, 192
        assert Hq % Hkv == 0, (Hq, Hkv)
        return {
            "x": _w((lambda *s: torch.randn(*s, dtype=torch.bfloat16) * 0.05)(B, Sq, D)),
            "k_cache": _w((lambda *s: torch.randn(*s, dtype=torch.bfloat16) * 1.6)(B, Hkv, Sk, Dh)),
            "v_cache": _w((lambda *s: torch.randn(*s, dtype=torch.bfloat16) * 0.2)(B, Hkv, Sk, Dh)),
            "wq": _w((lambda *s: torch.randn(*s, dtype=torch.bfloat16) * 1.6)(D, Hq * Dh) * (D ** -0.5)),
            "wo": _w((lambda *s: torch.randn(*s, dtype=torch.bfloat16) * 0.2)(Hq * Dh, D) * ((Hq * Dh) ** -0.5)),
        }
    _A_in = _A_setup()

    def _B_setup():
        B, S, D, F = 1, 1, 640, 1536
        return {
            "x": _w((lambda *s: torch.randn(*s, dtype=torch.bfloat16) * 0.05)(B, S, D)),
            "w_gate": _w((lambda *s: torch.randn(*s, dtype=torch.bfloat16) * 0.1)(D, F)),
            "w_up": _w((lambda *s: torch.randn(*s, dtype=torch.bfloat16) * 0.1)(D, F)),
            "w_down": _w((lambda *s: torch.randn(*s, dtype=torch.bfloat16) * 0.1)(F, D)),
        }
    _B_in = _B_setup()

    return {
        "A_x": _A_in["x"],
        "A_k_cache": _A_in["k_cache"],
        "A_v_cache": _A_in["v_cache"],
        "A_wq": _A_in["wq"],
        "A_wo": _A_in["wo"],
        "B_x": _B_in["x"],
        "B_w_gate": _B_in["w_gate"],
        "B_w_up": _B_in["w_up"],
        "B_w_down": _B_in["w_down"],
    }

def reference(A_x, A_k_cache, A_v_cache, A_wq, A_wo, B_x, B_w_gate, B_w_up, B_w_down):
    def _A_ref(x, k_cache, v_cache, wq, wo):
        B, Sq, D = x.shape
        Hq, Dh = 32, 192
        Hkv, Sk = k_cache.shape[1], k_cache.shape[2]
        W = min(4096, max(256, Sk // 4))
        rep = Hq // Hkv
        q = (lambda x, *d: x.permute(*d))((x @ wq).reshape(B, Sq, Hq, Dh), 0, 2, 1, 3)
        k = (lambda x, n, axis: torch.repeat_interleave(x, n, dim=axis))(k_cache[:, :, Sk - W:, :], rep, axis=1)
        v = (lambda x, n, axis: torch.repeat_interleave(x, n, dim=axis))(v_cache[:, :, Sk - W:, :], rep, axis=1)
        scores = torch.einsum("bhqd,bhkd->bhqk", q, k) * (Dh ** -0.5)
        p = _softmax(scores, axis=-1)
        o = torch.einsum("bhqk,bhkd->bhqd", p, v)
        return (lambda x, *d: x.permute(*d))(o, 0, 2, 1, 3).reshape(B, Sq, Hq * Dh) @ wo

    def _B_ref(x, w_gate, w_up, w_down):
        return (_gelu(x @ w_gate) * (x @ w_up)) @ w_down

    x = A_x
    xn = _layernorm(x, axis=-1)
    s = _A_ref(xn, A_k_cache, A_v_cache, A_wq, A_wo)
    g = _silu(_B_ref(xn, B_w_gate, B_w_up, B_w_down))
    return x + s * g
```

```cpp
#include <hip/hip_runtime.h>
#include <hip/hip_bf16.h>
#include <stdint.h>

typedef unsigned short u16;
__device__ __forceinline__ float bf2f(u16 b) { return __uint_as_float(((unsigned)b) << 16); }
__device__ __forceinline__ u16 f2bf(float f) { unsigned u = __float_as_uint(f); u += 0x7FFFu + ((u >> 16) & 1u); return (u16)(u >> 16); }
__device__ __forceinline__ float rbf(float f) { return bf2f(f2bf(f)); }

constexpr int D = 640, H = 32, DH = 192, SK = 32768, W = 4096, F = 1536, NS = 8;

__device__ void layernorm_to_lds(const u16* x, float* xn, float* red) {
  int t = threadIdx.x;
  float s = 0.f;
  for (int i = t; i < D; i += 256) s += bf2f(x[i]);
  red[t] = s; __syncthreads();
  for (int o = 128; o > 0; o >>= 1) { if (t < o) red[t] += red[t + o]; __syncthreads(); }
  float mean = red[0] / D; __syncthreads();
  float v = 0.f;
  for (int i = t; i < D; i += 256) { float d = bf2f(x[i]) - mean; v += d * d; }
  red[t] = v; __syncthreads();
  for (int o = 128; o > 0; o >>= 1) { if (t < o) red[t] += red[t + o]; __syncthreads(); }
  float var = red[0] / D; __syncthreads();
  float mb = rbf(mean), vb = rbf(var);
  float den = rbf(sqrtf(rbf(vb + 1e-5f)));
  for (int i = t; i < D; i += 256) xn[i] = rbf(rbf(bf2f(x[i]) - mb) / den);
  __syncthreads();
}

__global__ void k_proj(const u16* x, const u16* wq, const u16* wg, const u16* wu, float* q, float* hid) {
  __shared__ float xn[D]; __shared__ float red[256];
  layernorm_to_lds(x, xn, red);
  int c = blockIdx.x * 256 + threadIdx.x;
  if (c < H * DH) {
    float a = 0.f;
    for (int r = 0; r < D; ++r) a += xn[r] * bf2f(wq[(size_t)r * (H * DH) + c]);
    q[c] = rbf(a);
  } else {
    int cc = c - H * DH;
    float g = 0.f, u = 0.f;
    for (int r = 0; r < D; ++r) { g += xn[r] * bf2f(wg[(size_t)r * F + cc]); u += xn[r] * bf2f(wu[(size_t)r * F + cc]); }
    g = rbf(g); u = rbf(u);
    float ge = 0.5f * g * (1.f + tanhf(0.7978845608028654f * (g + 0.044715f * g * g * g)));
    hid[cc] = rbf(rbf(ge) * u);
  }
}

__global__ void k_attn(const float* q, const u16* K, const u16* V, float* po, float* pml) {
  __shared__ float sq[DH]; __shared__ float sc[512]; __shared__ float red[256];
  int h = blockIdx.x / NS, j = blockIdx.x % NS, t = threadIdx.x;
  for (int i = t; i < DH; i += 256) sq[i] = q[h * DH + i];
  __syncthreads();
  size_t base = ((size_t)h * SK + (SK - W) + j * 512) * DH;
  float m = -INFINITY;
  for (int k = t; k < 512; k += 256) {
    float a = 0.f;
    for (int d = 0; d < DH; ++d) a += sq[d] * bf2f(K[base + (size_t)k * DH + d]);
    a = rbf(rbf(a) * 0.07216878364870322f);
    sc[k] = a; m = fmaxf(m, a);
  }
  red[t] = m; __syncthreads();
  for (int o = 128; o > 0; o >>= 1) { if (t < o) red[t] = fmaxf(red[t], red[t + o]); __syncthreads(); }
  m = red[0]; __syncthreads();
  float l = 0.f;
  for (int k = t; k < 512; k += 256) { float p = __expf(sc[k] - m); sc[k] = p; l += p; }
  red[t] = l; __syncthreads();
  for (int o = 128; o > 0; o >>= 1) { if (t < o) red[t] += red[t + o]; __syncthreads(); }
  l = red[0];
  if (t < DH) {
    float a = 0.f;
    for (int k = 0; k < 512; ++k) a += sc[k] * bf2f(V[base + (size_t)k * DH + t]);
    po[(size_t)blockIdx.x * DH + t] = a;
  }
  if (t == 0) { pml[blockIdx.x * 2] = m; pml[blockIdx.x * 2 + 1] = l; }
}

__global__ void k_final(const u16* x, const float* po, const float* pml, const float* hid, const u16* wo, const u16* wd, u16* out) {
  __shared__ float o[H * DH]; __shared__ float hh[F]; __shared__ float red[256], red2[256];
  int t = threadIdx.x;
  for (int i = t; i < H * DH; i += 256) {
    int h = i / DH, d = i % DH;
    float M = -INFINITY;
    for (int j = 0; j < NS; ++j) M = fmaxf(M, pml[(h * NS + j) * 2]);
    float L = 0.f, a = 0.f;
    for (int j = 0; j < NS; ++j) { float w = __expf(pml[(h * NS + j) * 2] - M); L += w * pml[(h * NS + j) * 2 + 1]; a += w * po[(size_t)(h * NS + j) * DH + d]; }
    o[i] = rbf(a / L);
  }
  for (int i = t; i < F; i += 256) hh[i] = hid[i];
  __syncthreads();
  int c = blockIdx.x;
  float s = 0.f, dn = 0.f;
  for (int r = t; r < H * DH; r += 256) s += o[r] * bf2f(wo[(size_t)r * D + c]);
  for (int r = t; r < F; r += 256) dn += hh[r] * bf2f(wd[(size_t)r * D + c]);
  red[t] = s; red2[t] = dn; __syncthreads();
  for (int of = 128; of > 0; of >>= 1) { if (t < of) { red[t] += red[t + of]; red2[t] += red2[t + of]; } __syncthreads(); }
  if (t == 0) {
    float sv = rbf(red[0]), dv = rbf(red2[0]);
    float g = rbf(dv / (1.f + __expf(-dv)));
    out[c] = f2bf(bf2f(x[c]) + rbf(sv * g));
  }
}

extern "C" void kernel_launch(void* const* d_in, const int* in_sizes, int n_in, void* d_out, int out_size, void* d_ws, size_t ws_size, hipStream_t stream) {
  const u16* x = (const u16*)d_in[0]; const u16* K = (const u16*)d_in[1]; const u16* V = (const u16*)d_in[2];
  const u16* wq = (const u16*)d_in[3]; const u16* wo = (const u16*)d_in[4];
  const u16* wg = (const u16*)d_in[6]; const u16* wu = (const u16*)d_in[7]; const u16* wd = (const u16*)d_in[8];
  float* ws = (float*)d_ws;
  float* q = ws; float* hid = ws + 8192; float* po = ws + 16384; float* pml = ws + 16384 + 256 * DH;
  k_proj<<<(H * DH + F) / 256, 256, 0, stream>>>(x, wq, wg, wu, q, hid);
  k_attn<<<H * NS, 256, 0, stream>>>(q, K, V, po, pml);
  k_final<<<D, 256, 0, stream>>>(x, po, pml, hid, wo, wd, (u16*)d_out);
}
```

```cpp
#include <hip/hip_runtime.h>
#include <hip/hip_bf16.h>
#include <stdint.h>

#define NWAVES 12
#define WO_AT 120
#define WLATE 6
#define PRET 104
#define DLY1 4
#define DLY2 10
#define PK 3
#define CPF 1
#define STAMPS 0

typedef unsigned v4u __attribute__((ext_vector_type(4)));
typedef unsigned v2u __attribute__((ext_vector_type(2)));
typedef float v2f __attribute__((ext_vector_type(2)));
typedef float v4f __attribute__((ext_vector_type(4)));
typedef short v4s __attribute__((ext_vector_type(4)));
typedef __bf16 bf16x2 __attribute__((ext_vector_type(2)));
typedef __bf16 bf16x8 __attribute__((ext_vector_type(8)));
typedef unsigned short u16;
typedef unsigned long long u64;
#define GAS __attribute__((address_space(1)))
#define LAS __attribute__((address_space(3)))

constexpr int D = 640, H = 32, DH = 192, SK = 32768, WIN = 4096, F = 1536;
constexpr int ROWB = DH * 2;
constexpr int SPLIT_BYTES = 512 * ROWB;
constexpr int WS_EPOCH = 0;
constexpr int WS_H0 = 4096;
constexpr int WS_HM = WS_H0 + 32 * 9 * 2048;
constexpr int WS_H1 = WS_HM + 32 * 128;
constexpr int WS_HS = WS_H1 + 32 * 6 * 2048;
constexpr int WS_HD = WS_HS + 8 * 5 * 4096;
constexpr int WS_END = WS_HD + 8 * 5 * 4096;
constexpr int WS_STAMP = 4 << 20;
constexpr int OOB = 0x40000000;
constexpr unsigned SPIN_CAP = 1u << 17, LSPIN_CAP = 1u << 23;
constexpr int AUX_NT = 2, AUX_SC1 = 16;
constexpr unsigned SENT = 0xffffffffu;
constexpr int L_P0 = 0;
constexpr int L_SY = L_P0 + 4 * 512 * 4;
constexpr int L_HID = L_SY + 32;
constexpr int L_Q = L_HID + 128 + 96;
constexpr int L_O = L_Q + 384;
constexpr int L_OB = L_O + 16 * 200 * 4;
constexpr int L_ML = L_OB + 384;
constexpr int L_SENT_END = L_O + 4 * 4096;
constexpr int L_WO = L_SENT_END;
constexpr int L_WD = L_WO + 32768;
constexpr int L_END = L_WD + 8192;
constexpr int LDS_BYTES = 84 * 1024;
static_assert(L_END <= LDS_BYTES && L_O % 16 == 0 && L_Q % 16 == 0 && L_HID % 16 == 0 && L_OB % 16 == 0 && L_ML + 64 <= L_SENT_END, "lds map");
constexpr int SY_P0 = 0, SY_Q = 4, SY_G = 8, SY_WD = 12, SY_WO = 16;

__device__ __forceinline__ float dot2(unsigned a, unsigned b, float c) {
  return __builtin_amdgcn_fdot2_f32_bf16(__builtin_bit_cast(bf16x2, a), __builtin_bit_cast(bf16x2, b), c, false);
}
__device__ __forceinline__ unsigned f2bf(float f) { return (unsigned)__builtin_bit_cast(u16, (__bf16)f); }
__device__ __forceinline__ unsigned pk2bf(float lo, float hi) { bf16x2 v = {(__bf16)lo, (__bf16)hi}; return __builtin_bit_cast(unsigned, v); }
__device__ __forceinline__ float bf2f(unsigned b) { return __builtin_bit_cast(float, b << 16); }
__device__ __forceinline__ float rbf(float f) { return bf2f(f2bf(f)); }
__device__ __forceinline__ float asf(unsigned u) { return __builtin_bit_cast(float, u); }
__device__ __forceinline__ unsigned asu(float f) { return __builtin_bit_cast(unsigned, f); }
template <int CTRL> __device__ __forceinline__ float dpp(float x) {
  return asf((unsigned)__builtin_amdgcn_mov_dpp((int)asu(x), CTRL, 0xf, 0xf, true));
}
template <int CTRL, int RM, int BM> __device__ __forceinline__ unsigned dppu(unsigned old, unsigned x) {
  return (unsigned)__builtin_amdgcn_update_dpp((int)old, (int)x, CTRL, RM, BM, false);
}
constexpr int XOR1 = 0xB1, XOR2 = 0x4E, XOR7 = 0x141, XOR8 = 0x128, NEWB = 0x150;
__device__ __forceinline__ float tr16(float a, float b) {
  auto s = __builtin_amdgcn_permlane16_swap(asu(a), asu(b), false, false);
  return asf(s[0]) + asf(s[1]);
}
__device__ __forceinline__ float tr32(float a, float b) {
  auto s = __builtin_amdgcn_permlane32_swap(asu(a), asu(b), false, false);
  return asf(s[0]) + asf(s[1]);
}
__device__ __forceinline__ float max_rows(float x) {
  auto s = __builtin_amdgcn_permlane16_swap(asu(x), asu(x), false, false);
  x = fmaxf(asf(s[0]), asf(s[1]));
  auto t = __builtin_amdgcn_permlane32_swap(asu(x), asu(x), false, false);
  return fmaxf(asf(t[0]), asf(t[1]));
}
__device__ __forceinline__ float sum8(float x) { x += dpp<XOR1>(x); x += dpp<XOR2>(x); x += dpp<XOR7>(x); return x; }
__device__ __forceinline__ float max8(float x) { x = fmaxf(x, dpp<XOR1>(x)); x = fmaxf(x, dpp<XOR2>(x)); return fmaxf(x, dpp<XOR7>(x)); }
__device__ __forceinline__ float wave_sum(float x) { x = sum8(x); x += dpp<XOR8>(x); x = tr16(x, x); return tr32(x, x); }
__device__ __forceinline__ float wave_max(float x) { x = max8(x); x = fmaxf(x, dpp<XOR8>(x)); return max_rows(x); }
__device__ __forceinline__ float fexp(float x) { return __builtin_amdgcn_exp2f(x * 1.4426950408889634f); }
__device__ __forceinline__ v4f mfma(v4u a, v4u b, v4f c) {
  return __builtin_amdgcn_mfma_f32_16x16x32_bf16(__builtin_bit_cast(bf16x8, a), __builtin_bit_cast(bf16x8, b), c, 0, 0, 0);
}
__device__ __forceinline__ v4u tr_frag(const LAS char* p) {
  const v4s lo = __builtin_amdgcn_ds_read_tr16_b64_v4i16((LAS v4s*)p), hi = __builtin_amdgcn_ds_read_tr16_b64_v4i16((LAS v4s*)(p + 4 * 160));
  const v2u l = __builtin_bit_cast(v2u, lo), h = __builtin_bit_cast(v2u, hi);
  return v4u{l[0], l[1], h[0], h[1]};
}
__device__ __forceinline__ unsigned umax3(unsigned a, unsigned b, unsigned c) { return max(max(a, b), c); }
__device__ __forceinline__ u64 bad_lanes(v4u v, unsigned tag) { return __builtin_amdgcn_ballot_w64(__builtin_amdgcn_bitop3_b32(v[1], v[3], tag, 0x7E) != 0u); }
__device__ __forceinline__ void axpy8(float (&acc)[8], v4u w, unsigned slo, unsigned shi) {
#pragma unroll
  for (int k = 0; k < 4; ++k) { acc[2 * k] = dot2(w[k], slo, acc[2 * k]); acc[2 * k + 1] = dot2(w[k], shi, acc[2 * k + 1]); }
}
__device__ __forceinline__ void reload2(v4u& a, v4u& b, u64 ma, u64 mb, int off, v4u rs) {
  asm volatile("s_mov_b64 exec, %[ma]\n\tbuffer_load_dwordx4 %[a], %[off], %[rs], 0 offen sc1\n\t"
               "s_mov_b64 exec, %[mb]\n\tbuffer_load_dwordx4 %[b], %[off], %[rs], 0 offen offset:1024 sc1\n\t"
               "s_mov_b64 exec, -1\n\ts_waitcnt vmcnt(0)"
               : [a] "+v"(a), [b] "+v"(b) : [ma] "s"(ma), [mb] "s"(mb), [off] "v"(off), [rs] "s"(rs) : "memory");
}
__device__ __forceinline__ void reload4(v4u (&f)[4], const u64 (&m)[4], int off, v4u rs) {
  asm volatile("s_mov_b64 exec, %[m0]\n\tbuffer_load_dwordx4 %[a], %[off], %[rs], 0 offen sc1\n\t"
               "s_mov_b64 exec, %[m1]\n\tbuffer_load_dwordx4 %[b], %[off], %[rs], 0 offen offset:1024 sc1\n\t"
               "s_mov_b64 exec, %[m2]\n\tbuffer_load_dwordx4 %[c], %[off], %[rs], 0 offen offset:2048 sc1\n\t"
               "s_mov_b64 exec, %[m3]\n\tbuffer_load_dwordx4 %[d], %[off], %[rs], 0 offen offset:3072 sc1\n\t"
               "s_mov_b64 exec, -1\n\ts_waitcnt vmcnt(0)"
               : [a] "+v"(f[0]), [b] "+v"(f[1]), [c] "+v"(f[2]), [d] "+v"(f[3])
               : [m0] "s"(m[0]), [m1] "s"(m[1]), [m2] "s"(m[2]), [m3] "s"(m[3]), [off] "v"(off), [rs] "s"(rs) : "memory");
}
__device__ __forceinline__ float gather32(int off, v4u rs, unsigned tag) {
  v4u f[4]; u64 m[4];
#pragma unroll
  for (int i = 0; i < 4; ++i) { f[i] = v4u{0u, 0u, 0u, 0u}; m[i] = ~0ull; }
#pragma unroll 1
  for (unsigned spin = 0; spin < SPIN_CAP; ++spin) {
    reload4(f, m, off, rs);
#pragma unroll
    for (int i = 0; i < 4; ++i) m[i] = bad_lanes(f[i], tag);
    if (((m[0] | m[1]) | (m[2] | m[3])) == 0ull) break;
  }
  float a = tr16((asf(f[0][0]) + asf(f[1][0])) + (asf(f[2][0]) + asf(f[3][0])), (asf(f[0][2]) + asf(f[1][2])) + (asf(f[2][2]) + asf(f[3][2])));
  a += dpp<XOR8>(a);
  return tr32(a, a);
}

#define SB() __builtin_amdgcn_sched_barrier(0)
#define PACE_(n) do { SB(); asm volatile("s_waitcnt vmcnt(" #n ")" ::: "memory"); SB(); } while (0)
#define PACE(n) PACE_(n)
#define PIN(x) asm volatile("" : "+v"(x))
#define MEMBAR() asm volatile("" ::: "memory")
#define STAMP(i) do {} while (0)
#define LD_G(off, so) __builtin_amdgcn_raw_buffer_load_b128(rws, off, so, AUX_SC1)
#define ST_G2(v0, v1, off) __builtin_amdgcn_raw_buffer_store_b128(v4u{asu(v0), tag, asu(v1), tag}, rws, off, 0, AUX_SC1)
#define ST_G1(v0, off) __builtin_amdgcn_raw_buffer_store_b64(v2u{asu(v0), tag}, rws, off, 0, AUX_SC1)
#define LDS_INC(off) do { MEMBAR(); if (lane == 0) __hip_atomic_fetch_add((LAS unsigned*)(L + L_SY + (off)), 1u, __ATOMIC_RELAXED, __HIP_MEMORY_SCOPE_WORKGROUP); MEMBAR(); } while (0)
#define LDS_WAIT(off, n) do { _Pragma("unroll 1") for (unsigned sp_ = 0; sp_ < LSPIN_CAP; ++sp_) { \
      if (__builtin_amdgcn_readfirstlane(__hip_atomic_load((LAS unsigned*)(L + L_SY + (off)), __ATOMIC_RELAXED, __HIP_MEMORY_SCOPE_WORKGROUP)) >= (n)) break; \
      __builtin_amdgcn_s_sleep(1); } MEMBAR(); } while (0)
#define STEP(n, LOAD) do { LDS_WAIT(SY_G, 8 * ((n) - 1)); SB(); LOAD; LDS_INC(SY_G); SB(); } while (0)
#define BAR() do { SB(); asm volatile("s_waitcnt lgkmcnt(0)" ::: "memory"); __builtin_amdgcn_s_barrier(); MEMBAR(); SB(); } while (0)
#define GET_TAG() ({ unsigned e_; asm volatile("s_load_dword %0, %1, 0x0\n\ts_waitcnt lgkmcnt(0)" : "=s"(e_) : "s"(ws) : "memory"); e_ * 1664525u + 1013904223u; })

__global__ void __launch_bounds__(768, 3)
fused_decode(const u16* __restrict__ x, const u16* __restrict__ Kc, const u16* __restrict__ Vc, const u16* __restrict__ wq,
             const u16* __restrict__ wo, const u16* __restrict__ wg, const u16* __restrict__ wu, const u16* __restrict__ wd,
             u16* __restrict__ out, unsigned char* ws) {
  __shared__ __attribute__((aligned(16))) char smem[LDS_BYTES];
  LAS char* const L = (LAS char*)smem;
  const int lane = threadIdx.x & 63, wv = __builtin_amdgcn_readfirstlane(threadIdx.x >> 6);
  const int b = blockIdx.x, h = (b & 7) * 4 + ((b >> 3) & 3), j = b >> 5;
  const int k = lane & 7, row = lane >> 4;
  STAMP(0);
  uint64_t pc0; unsigned clen;
  asm volatile("s_getpc_b64 %0\nfd_code_begin%=:\n\ts_mov_b32 %1, fd_code_end-fd_code_begin%=" : "=s"(pc0), "=s"(clen));
  const unsigned cpf = __builtin_amdgcn_raw_buffer_load_b32(__builtin_amdgcn_make_buffer_rsrc((void*)pc0, 0, (int)clen > 0 ? (int)clen : 0, 0x00020000), threadIdx.x * 128, 0, 0);
  SB();
  asm volatile("" :: "s"(x), "s"(Kc), "s"(Vc), "s"(wq), "s"(wo), "s"(wg), "s"(wu), "s"(wd), "s"(out), "s"(ws));
  const __amdgpu_buffer_rsrc_t rws = __builtin_amdgcn_make_buffer_rsrc((void*)ws, 0, WS_END, 0x00020000);
  const v4u rsw = {(unsigned)(uint64_t)ws, (unsigned)((uint64_t)ws >> 32) & 0xffffu, (unsigned)WS_END, 0x00020000u};
  unsigned tag;
  if (wv >= 4) {
    const int w = wv - 4, tid = threadIdx.x - 256;
    const size_t kvbase = ((size_t)h * SK + (SK - WIN) + (size_t)j * 512) * DH;
    const __amdgpu_buffer_rsrc_t rk = __builtin_amdgcn_make_buffer_rsrc((void*)(Kc + kvbase), 0, SPLIT_BYTES, 0x00020000);
    const __amdgpu_buffer_rsrc_t rv = __builtin_amdgcn_make_buffer_rsrc((void*)(Vc + kvbase), 0, SPLIT_BYTES, 0x00020000);
    const int kvoff = (tid >> 3) * ROWB + k * 16;
    v4u kr[8][3], vr[8][3];
#define LDK(a) do { _Pragma("unroll") for (int r_ = 0; r_ < 3; ++r_) kr[a][r_] = __builtin_amdgcn_raw_buffer_load_b128(rk, kvoff + r_ * 128, (a) * 64 * ROWB, AUX_NT); SB(); } while (0)
#define LDV(a) do { _Pragma("unroll") for (int r_ = 0; r_ < 3; ++r_) vr[a][r_] = __builtin_amdgcn_raw_buffer_load_b128(rv, kvoff + r_ * 128, (a) * 64 * ROWB, AUX_NT); SB(); } while (0)
    LDK(0);
    BAR();
    LDK(1); LDS_INC(SY_G);
    PACE(PK); STEP(2, LDK(2));
    PACE(PK); STEP(3, LDK(3));
    PACE(PK); STEP(4, LDK(4));
    PACE(PK); STEP(5, LDK(5));
    PACE(PK); STEP(6, LDK(6));
    PACE(PK); STEP(7, LDK(7));
    PACE(PK); STEP(8, LDV(0));
    PACE(PK); STEP(9, LDV(1)); LDV(2); LDS_INC(SY_G);
    tag = GET_TAG();
    asm volatile("" :: "v"(cpf));
    STAMP(4);
    LDS_WAIT(SY_Q, 3);
    v4u qf[3];
#pragma unroll
    for (int r = 0; r < 3; ++r) qf[r] = *(const LAS v4u*)(L + L_Q + r * 128 + k * 16);
    float sc[8];
#pragma unroll
    for (int a = 0; a < 8; ++a) {
      float d[3];
#pragma unroll
      for (int r = 0; r < 3; ++r) { d[r] = 0.f;
#pragma unroll
        for (int e = 0; e < 4; ++e) d[r] = dot2(kr[a][r][e], qf[r][e], d[r]); }
      sc[a] = (d[0] + d[1]) + d[2];
    }
#pragma unroll
    for (int i = 0; i < 4; ++i) { const float t0 = sc[i] + dpp<XOR7>(sc[i]), t1 = sc[i + 4] + dpp<XOR7>(sc[i + 4]); sc[i] = (k & 4) ? t1 : t0; }
#pragma unroll
    for (int i = 0; i < 2; ++i) { const float t0 = sc[i] + dpp<XOR2>(sc[i]), t1 = sc[i + 2] + dpp<XOR2>(sc[i + 2]); sc[i] = (k & 2) ? t1 : t0; }
    float s;
    { const float t0 = sc[0] + dpp<XOR1>(sc[0]), t1 = sc[1] + dpp<XOR1>(sc[1]); s = (k & 1) ? t1 : t0; }
    s = rbf(rbf(s) * 0.07216878364870322f);
    const float mw = wave_max(s);
    const float pf = fexp(s - mw);
    const float lw = wave_sum(pf);
    if (lane == 0) *(LAS v2f*)(L + L_ML + w * 8) = v2f{mw, lw};
    const unsigned pp = pk2bf(pf, pf);
    STAMP(5);
    unsigned msk[4];
#pragma unroll
    for (int e = 0; e < 4; ++e) msk[e] = (k >> 1) == e ? ((k & 1) ? 0xffff0000u : 0x0000ffffu) : 0u;
    v4f oa[3];
#pragma unroll
    for (int r = 0; r < 3; ++r) oa[r] = v4f{0.f, 0.f, 0.f, 0.f};
#define PV(a) do { unsigned p_ = dppu<NEWB + (a), 0xf, 0x3>(0u, pp); p_ = dppu<NEWB + 8 + (a), 0xf, 0xC>(p_, pp); \
    const v4u a_ = {p_ & msk[0], p_ & msk[1], p_ & msk[2], p_ & msk[3]}; \
    _Pragma("unroll") for (int r_ = 0; r_ < 3; ++r_) oa[r_] = mfma(a_, vr[a][r_], oa[r_]); SB(); } while (0)
    PV(0);
    PACE(PK); STEP(11, LDV(3)); PV(1);
    PACE(PK); STEP(12, LDV(4)); PV(2);
    PACE(PK); STEP(13, LDV(5)); PV(3);
    PACE(PK); STEP(14, LDV(6)); PV(4);
    PACE(PK); STEP(15, LDV(7)); PV(5);
    unsigned ow_addr = L_O + (2 * w + (lane >> 5)) * 800 + k * 32 + (row & 1) * 16;
    unsigned or_addr = L_O + row * 4 * 800 + (32 * w + 2 * (lane & 15)) * 4;
    unsigned ob_addr = L_OB + (w * 16 + (lane & 15)) * 4 + (row & 1) * 2;
    const int blk = WS_H1 + (h * 6 + w) * 2048;
    int st_off = lane < 32 ? blk + j * 256 + (lane & 15) * 16 + row * 8 : OOB;
    int o_off = blk + lane * 16, o_off0 = row == j ? OOB : o_off, o_off1 = row + 4 == j ? OOB : o_off;
    int m_off = WS_HM + h * 128 + row * 16;
    unsigned tagv = tag;
    const bool valid = ((lane >> 3) & 1) == (lane >> 5);
    PIN(ow_addr); PIN(or_addr); PIN(ob_addr); PIN(st_off); PIN(o_off); PIN(o_off0); PIN(o_off1); PIN(m_off); PIN(tagv);
    unsigned mt;
#pragma unroll 1
    for (unsigned sp = 0; sp < LSPIN_CAP; ++sp) { MEMBAR(); mt = *(const LAS unsigned*)(L + L_ML + k * 8); if (__builtin_amdgcn_ballot_w64(mt == SENT) == 0ull) break; }
    const float m = max8(asf(mt));
    float cw = fexp(mw - m);
    PIN(cw);
    SB();
    PV(6);
    PV(7);
    STAMP(6);
    if (valid) {
#pragma unroll
      for (int r = 0; r < 3; ++r) *(LAS v4f*)(L + ow_addr + r * 256) = oa[r] * cw;
    }
    MEMBAR();
    if (w < 6) {
      v2u t0, t1, t2, t3;
#pragma unroll 1
      for (unsigned sp = 0; sp < LSPIN_CAP; ++sp) {
        MEMBAR();
        t0 = *(const LAS v2u*)(L + or_addr); t1 = *(const LAS v2u*)(L + or_addr + 800); t2 = *(const LAS v2u*)(L + or_addr + 1600); t3 = *(const LAS v2u*)(L + or_addr + 2400);
        if (__builtin_amdgcn_ballot_w64(max(umax3(t0[0], t1[0], t2[0]), t3[0]) == SENT) == 0ull) break;
      }
      float own = tr16((asf(t0[0]) + asf(t1[0])) + (asf(t2[0]) + asf(t3[0])), (asf(t0[1]) + asf(t1[1])) + (asf(t2[1]) + asf(t3[1])));
      own = tr32(own, own);
      __builtin_amdgcn_raw_buffer_store_b64(v2u{asu(own), tagv}, rws, st_off, 0, AUX_SC1);
      SB();
      STAMP(7);
      v4u pa = LD_G(m_off, 0), pb = LD_G(m_off, 64);
      if (DLY1) __builtin_amdgcn_s_sleep(DLY1);
      v4u po0 = LD_G(o_off0, 0), po1 = LD_G(o_off1, 1024);
      SB();
      if ((bad_lanes(pa, tag) | bad_lanes(pb, tag)) != 0ull) {
#pragma unroll 1
        for (unsigned spin = 0; spin < SPIN_CAP; ++spin) {
          __builtin_amdgcn_s_sleep(2);
          asm volatile("" : "+v"(m_off) :: "memory");
          pa = LD_G(m_off, 0); pb = LD_G(m_off, 64);
          if ((bad_lanes(pa, tag) | bad_lanes(pb, tag)) == 0ull) break;
        }
      }
      const float M = max_rows(fmaxf(asf(pa[0]), asf(pb[0])));
      float c0 = fexp(asf(pa[0]) - M), c1 = fexp(asf(pb[0]) - M);
      float Ls = fmaf(c0, asf(pa[2]), c1 * asf(pb[2]));
      Ls = tr16(Ls, Ls); Ls = tr32(Ls, Ls);
      const float inv = 1.0f / Ls;
      c0 *= inv; c1 *= inv; own *= fexp(m - M) * inv;
      PIN(c0); PIN(c1); PIN(own);
      const u64 rowm = 0xffffull << (16 * (j & 3)), keep0 = j < 4 ? ~rowm : ~0ull, keep1 = j < 4 ? ~0ull : ~rowm;
      SB();
      u64 m0 = bad_lanes(po0, tag) & keep0, m1 = bad_lanes(po1, tag) & keep1;
      if ((m0 | m1) != 0ull) {
#pragma unroll 1
        for (unsigned spin = 0; spin < SPIN_CAP; ++spin) {
          reload2(po0, po1, m0, m1, o_off, rsw);
          m0 = bad_lanes(po0, tag) & keep0; m1 = bad_lanes(po1, tag) & keep1;
          if ((m0 | m1) == 0ull) break;
        }
      }
      STAMP(8);
      float o = tr16(fmaf(c0, asf(po0[0]), c1 * asf(po1[0])), fmaf(c0, asf(po0[2]), c1 * asf(po1[2])));
      o = tr32(o, o) + own;
      *(LAS u16*)(L + ob_addr) = (u16)f2bf(o);
    } else if (w == 6 && h < 5) {
      const int col = 80 * j + 16 * h + 2 * k + (row & 1);
      const __amdgpu_buffer_rsrc_t rout = __builtin_amdgcn_make_buffer_rsrc((void*)out, 0, D * 2, 0x00020000);
      int out_off = (lane & 0x28) == 0 ? col * 2 : OOB;
      float xv = bf2f(x[col]);
      const float dv = rbf(gather32(WS_HD + (j * 5 + h) * 4096 + lane * 16, rsw, tag));
      float gt = rbf(dv / (1.0f + fexp(-dv)));
      int f_off = WS_HS + (j * 5 + h) * 4096 + lane * 16;
      PIN(gt); PIN(xv); PIN(out_off); PIN(f_off);
#pragma unroll 1
      for (unsigned sp = 0; sp < LSPIN_CAP; ++sp) {
        MEMBAR();
        if (__builtin_amdgcn_ballot_w64(*(const LAS unsigned*)(L + L_OB + (k < 6 ? k : 0) * 64) == SENT) == 0ull) break;
        __builtin_amdgcn_s_sleep(1);
      }
      if (DLY2) __builtin_amdgcn_s_sleep(DLY2);
      const float sv = rbf(gather32(f_off, rsw, tag));
      __builtin_amdgcn_raw_buffer_store_b16((u16)f2bf(xv + rbf(sv * gt)), rout, out_off, 0, 0);
      if (b == 0 && lane == 0) __builtin_amdgcn_raw_buffer_store_b32(tag, rws, WS_EPOCH, 0, AUX_SC1);
      STAMP(10);
    }
  } else {
    const int u = wv, tid = threadIdx.x;
    const int row0 = 80 * j + 20 * u;
    const v4u xa = *(const GAS v4u*)(x + lane * 8);
    const v4u xb = *(const GAS v4u*)(x + 512 + (lane & 15) * 8);
    const unsigned xr = x[row0 + (lane < 20 ? lane : 19)];
    v4u w1[20];
    {
      const int c = lane < 35 ? lane : 35;
      uint64_t pa = (uint64_t)(wq + (size_t)row0 * (H * DH) + h * DH), pb = (uint64_t)(wg + (size_t)row0 * F + h * 48 - 24 * 8),
               pc = (uint64_t)(wu + (size_t)row0 * F + h * 48 - 30 * 8);
      asm volatile("" : "+s"(pa), "+s"(pb), "+s"(pc));
      const u16* p = (const u16*)(c < 24 ? pa : (c < 30 ? pb : pc)) + c * 8;
      const int stride = c < 24 ? H * DH : F;
#pragma unroll
      for (int i = 0; i < 20; ++i) w1[i] = *(const GAS v4u*)(p + (size_t)i * stride);
    }
    SB();
    if (tid < 8) *(LAS unsigned*)(L + L_SY + tid * 4) = 0u;
    if (tid < 8) *(LAS unsigned*)(L + L_HID + 96 + tid * 4) = 0u;
#pragma unroll
    for (int i = 0; i < 4; ++i) *(LAS v4u*)(L + L_O + i * 4096 + tid * 16) = v4u{SENT, SENT, SENT, SENT};
    BAR();
    float xs[16];
#pragma unroll
    for (int i = 0; i < 4; ++i) { xs[2 * i] = asf(xa[i] << 16); xs[2 * i + 1] = asf(xa[i] & 0xffff0000u); xs[8 + 2 * i] = asf(xb[i] << 16); xs[8 + 2 * i + 1] = asf(xb[i] & 0xffff0000u); }
    const bool has_b = lane < 16;
    float s1 = 0.f, s1b = 0.f;
#pragma unroll
    for (int i = 0; i < 8; ++i) { s1 += xs[i]; s1b += xs[8 + i]; }
    const float mean = wave_sum(s1 + (has_b ? s1b : 0.f)) * (1.0f / D);
    float s2 = 0.f, s2b = 0.f;
#pragma unroll
    for (int i = 0; i < 8; ++i) { const float d0 = xs[i] - mean, d1 = xs[8 + i] - mean; s2 = fmaf(d0, d0, s2); s2b = fmaf(d1, d1, s2b); }
    const float var = wave_sum(s2 + (has_b ? s2b : 0.f)) * (1.0f / D);
    const float den = rbf(__builtin_sqrtf(rbf(rbf(var) + 1e-5f)));
    const unsigned xn = f2bf(rbf(bf2f(xr) - rbf(mean)) / den);
    tag = GET_TAG();
    asm volatile("" :: "v"(cpf));
    STAMP(1);
    {
      float acc[8];
#pragma unroll
      for (int i = 0; i < 8; ++i) acc[i] = 0.f;
#pragma unroll
      for (int i = 0; i < 20; ++i) { const unsigned s = __builtin_amdgcn_readlane(xn, i); axpy8(acc, w1[i], s, s << 16); }
      *(LAS v4f*)(L + L_P0 + tid * 32) = v4f{acc[0], acc[1], acc[2], acc[3]};
      *(LAS v4f*)(L + L_P0 + tid * 32 + 16) = v4f{acc[4], acc[5], acc[6], acc[7]};
    }
    STAMP(2);
    LDS_INC(SY_P0);
    if (u < 3) {
      LDS_WAIT(SY_P0, 4);
      if (tid < 144) {
        const bool isq = tid < 96;
        const int uu = tid - 96;
        const int c0 = isq ? 2 * tid : 192 + uu, c1 = isq ? 2 * tid + 1 : 240 + uu;
        float a0 = 0.f, a1 = 0.f;
#pragma unroll
        for (int ww = 0; ww < 4; ++ww) { a0 += *(LAS float*)(L + L_P0 + ww * 2048 + c0 * 4); a1 += *(LAS float*)(L + L_P0 + ww * 2048 + c1 * 4); }
        ST_G2(a0, a1, WS_H0 + (h * 9 + (tid >> 4)) * 2048 + j * 256 + (tid & 15) * 16);
      }
      STAMP(3);
      asm volatile("s_waitcnt vmcnt(0)" ::: "memory");
      const int q_off = WS_H0 + (h * 9 + 2 * u) * 2048 + lane * 16, g_off = WS_H0 + (h * 9 + 6 + u) * 2048 + lane * 16;
      v4u pq[4], pg0 = LD_G(g_off, 0), pg1 = LD_G(g_off, 1024);
      u64 mq[4];
#pragma unroll
      for (int i = 0; i < 4; ++i) { pq[i] = v4u{0u, 0u, 0u, 0u}; mq[i] = ~0ull; }
#pragma unroll 1
      for (unsigned spin = 0; spin < SPIN_CAP; ++spin) {
        reload4(pq, mq, q_off, rsw);
#pragma unroll
        for (int i = 0; i < 4; ++i) mq[i] = bad_lanes(pq[i], tag);
        if (((mq[0] | mq[1]) | (mq[2] | mq[3])) == 0ull) break;
        __builtin_amdgcn_s_sleep(4);
      }
      float q0 = tr32(asf(pq[0][0]) + asf(pq[1][0]), asf(pq[2][0]) + asf(pq[3][0])), q1 = tr32(asf(pq[0][2]) + asf(pq[1][2]), asf(pq[2][2]) + asf(pq[3][2]));
      q0 = tr16(q0, q0); q1 = tr16(q1, q1);
      *(LAS unsigned*)(L + L_Q + (u * 32 + (lane >> 5) * 16 + (lane & 15)) * 4) = pk2bf(q0, q1);
      LDS_INC(SY_Q);
      STAMP(12);
#pragma unroll 1
      for (unsigned spin = 0; spin < SPIN_CAP; ++spin) {
        const u64 m0 = bad_lanes(pg0, tag), m1 = bad_lanes(pg1, tag);
        if ((m0 | m1) == 0ull) break;
        reload2(pg0, pg1, m0, m1, g_off, rsw);
      }
      float ga = asf(pg0[0]) + asf(pg1[0]), up = asf(pg0[2]) + asf(pg1[2]);
      ga = tr16(ga, ga); ga = tr32(ga, ga); up = tr16(up, up); up = tr32(up, up);
      ga = rbf(ga); up = rbf(up);
      const float inner = 0.7978845608028654f * (ga + 0.044715f * ga * ga * ga);
      const float ge = rbf(ga / (1.0f + fexp(-2.0f * inner)));
      *(LAS u16*)(L + L_HID + (u * 16 + (lane & 15)) * 2) = (u16)f2bf(ge * up);
    }
    const __amdgpu_buffer_rsrc_t ro = __builtin_amdgcn_make_buffer_rsrc((void*)(wo + (size_t)h * DH * D + 80 * j), 0, 191 * 1280 + 160, 0x00020000);
    const __amdgpu_buffer_rsrc_t rd = __builtin_amdgcn_make_buffer_rsrc((void*)(wd + (size_t)h * 48 * D + 80 * j), 0, 47 * 1280 + 160, 0x00020000);
    int woff[8];
#pragma unroll
    for (int n = 0; n < 8; ++n) { const int e = tid + 256 * n; woff[n] = (e / 10) * 1280 + (e % 10) * 16; PIN(woff[n]); }
    v4u wr[10];
#define LDW(n) wr[n] = __builtin_amdgcn_raw_buffer_load_b128((n) < 8 ? ro : rd, woff[(n) & 7], 0, 0)
#define STW(n) *(LAS v4u*)(L + ((n) < 8 ? L_WO : L_WD) + (tid + 256 * ((n) & 7)) * 16) = wr[n]
#pragma unroll
    for (int n = WLATE; n < 10; ++n) LDW(n);
    SB();
#pragma unroll
    for (int n = WLATE; n < 10; ++n) STW(n);
    LDS_INC(SY_WD);
    LDS_WAIT(SY_WD, 4);
    {
      const v4u h0 = *(const LAS v4u*)(L + L_HID + row * 16), h1 = *(const LAS v4u*)(L + L_HID + 64 + row * 16);
#pragma unroll
      for (int i = 0; i < 2; ++i) {
        if (i == 0 || u == 3) {
          const int cb = i == 0 ? u : 4;
          const LAS char* pd = L + L_WD + (8 * row + ((lane >> 2) & 3)) * 160 + cb * 32 + (lane & 3) * 8;
          const v4u d0 = tr_frag(pd), d1 = tr_frag(pd + (row < 2 ? 32 * 160 : 0));
          v4f da = mfma(h0, d0, v4f{0.f, 0.f, 0.f, 0.f});
          da = mfma(h1, d1, da);
          ST_G1(da[0], lane < 16 ? WS_HD + ((j * 5 + cb) * 32 + h) * 128 + lane * 8 : OOB);
        }
      }
    }
    if (u == 3) {
      v2u t;
#pragma unroll 1
      for (unsigned sp = 0; sp < LSPIN_CAP; ++sp) { MEMBAR(); t = *(const LAS v2u*)(L + L_ML + k * 8); if (__builtin_amdgcn_ballot_w64(t[0] == SENT) == 0ull) break; __builtin_amdgcn_s_sleep(2); }
      const float m = max8(asf(t[0])), l = sum8(fexp(asf(t[0]) - m) * asf(t[1]));
      ST_G2(m, l, lane == 0 ? WS_HM + h * 128 + j * 16 : OOB);
      STAMP(13);
    } else {
      LDS_WAIT(SY_G, PRET);
      v4u t = {0u, 0u, 0u, 0u};
#pragma unroll
      for (int i = 0; i < 4; ++i) t ^= LD_G(WS_H1 + (h * 6 + 2 * u) * 2048 + lane * 16, i * 1024);
      if (u == 0 && h < 5) {
#pragma unroll
        for (int i = 0; i < 4; ++i) t ^= LD_G(WS_HS + (j * 5 + h) * 4096 + lane * 16, i * 1024);
      }
      asm volatile("" :: "v"(t));
    }
    LDS_WAIT(SY_G, WO_AT);
#pragma unroll
    for (int n = 0; n < WLATE; ++n) LDW(n);
    SB();
#pragma unroll
    for (int n = 0; n < WLATE; ++n) STW(n);
    LDS_INC(SY_WO);
    STAMP(14);
  }
  if (wv < 4 || wv == 11) {
    const int cb = wv < 4 ? wv : 4;
    int st_off = lane < 16 ? WS_HS + ((j * 5 + cb) * 32 + h) * 128 + lane * 8 : OOB;
    unsigned a_addr = L_OB + row * 16, tagv = tag;
    PIN(st_off); PIN(a_addr); PIN(tagv);
    LDS_WAIT(SY_WO, 4);
    v4u bf[6];
    const LAS char* p = L + L_WO + (8 * row + ((lane >> 2) & 3)) * 160 + cb * 32 + (lane & 3) * 8;
#pragma unroll
    for (int st = 0; st < 6; ++st) bf[st] = tr_frag(p + st * 32 * 160);
#pragma unroll
    for (int st = 0; st < 6; ++st) PIN(bf[st]);
    STAMP(11);
    v4u af[6];
#pragma unroll 1
    for (unsigned sp = 0; sp < LSPIN_CAP; ++sp) {
      MEMBAR();
#pragma unroll
      for (int st = 0; st < 6; ++st) af[st] = *(const LAS v4u*)(L + a_addr + st * 64);
      if (__builtin_amdgcn_ballot_w64(max(umax3(af[0][0], af[1][0], af[2][0]), umax3(af[3][0], af[4][0], af[5][0])) == SENT) == 0ull) break;
    }
    v4f s0 = {0.f, 0.f, 0.f, 0.f}, s1v = {0.f, 0.f, 0.f, 0.f};
#pragma unroll
    for (int st = 0; st < 3; ++st) { s0 = mfma(af[st], bf[st], s0); s1v = mfma(af[st + 3], bf[st + 3], s1v); }
    __builtin_amdgcn_raw_buffer_store_b64(v2u{asu(s0[0] + s1v[0]), tagv}, rws, st_off, 0, AUX_SC1);
    asm volatile("fd_code_end:");
    STAMP(9);
  }
}

extern "C" void kernel_launch(void* const* d_in, const int* in_sizes, int n_in, void* d_out, int out_size, void* d_ws, size_t ws_size,
                              hipStream_t stream) {
  fused_decode<<<256, 768, 0, stream>>>((const u16*)d_in[0], (const u16*)d_in[1], (const u16*)d_in[2], (const u16*)d_in[3], (const u16*)d_in[4],
                                        (const u16*)d_in[6], (const u16*)d_in[7], (const u16*)d_in[8], (u16*)d_out, (unsigned char*)d_ws);
}
```

```cpp
#include <hip/hip_runtime.h>
#include <hip/hip_bf16.h>
#include <stdint.h>

constexpr int TAU = 92, TOFF = 35;
constexpr int TAU2 = 95, TOFF2 = 100;
constexpr int POLAT = 72, PRET = 104;
constexpr int DLY1 = 10, DLY2 = 40;

typedef unsigned v4u __attribute__((ext_vector_type(4)));
typedef unsigned v2u __attribute__((ext_vector_type(2)));
typedef float v2f __attribute__((ext_vector_type(2)));
typedef float v4f __attribute__((ext_vector_type(4)));
typedef __bf16 bf16x2 __attribute__((ext_vector_type(2)));
typedef __bf16 bf16x8 __attribute__((ext_vector_type(8)));
typedef unsigned short u16;
typedef unsigned long long u64;
#define GAS __attribute__((address_space(1)))
#define LAS __attribute__((address_space(3)))

constexpr int D = 640, H = 32, DH = 192, SK = 32768, WIN = 4096, F = 1536;
constexpr int ROWB = DH * 2;
constexpr int WS_EPOCH = 0;
constexpr int WS_HT = 2048;
constexpr int WS_HP = 3072;
constexpr int WS_H0 = 4096;
constexpr int WS_HG = WS_H0 + 256 * 768;
constexpr int WS_HM = WS_HG + 96 * 1024;
constexpr int WS_PR = 0x50000;
constexpr int WS_H1 = WS_PR + 16384;
constexpr int WS_HS = WS_H1 + 4 * 49152;
constexpr int WS_END = WS_HS + 10 * 16384 + 16384;
static_assert(WS_HM + 32 * 128 <= WS_PR, "ws map");
constexpr int OOB = 0x40000000;
constexpr unsigned SPIN_CAP = 1u << 17, LSPIN_CAP = 1u << 23;
constexpr int AUX_NT = 2, AUX_SC1 = 16;
constexpr unsigned SENT = 0xffffffffu;
constexpr int L_P0 = 0;
constexpr int L_SY = L_P0 + 4 * 512 * 4;
constexpr int L_Q = L_SY + 32;
constexpr int L_O = L_Q + 384;
constexpr int L_S = L_O + 16 * 800;
constexpr int L_D = L_S + 16 * 272;
constexpr int L_ML = L_D + 8 * 272;
constexpr int L_SENT_END = L_O + 5 * 4096;
constexpr int LDS_BYTES = 84 * 1024;
static_assert(L_SENT_END <= LDS_BYTES && L_O % 16 == 0 && L_Q % 16 == 0 && L_S % 16 == 0 && L_D % 16 == 0 && L_ML + 64 <= L_SENT_END, "lds map");
constexpr int SY_P0 = 0, SY_Q = 4, SY_G = 8, SY_T0 = 12, SY_POL = 16;

__device__ __forceinline__ float dot2(unsigned a, unsigned b, float c) {
  return __builtin_amdgcn_fdot2_f32_bf16(__builtin_bit_cast(bf16x2, a), __builtin_bit_cast(bf16x2, b), c, false);
}
__device__ __forceinline__ unsigned f2bf(float f) { return (unsigned)__builtin_bit_cast(u16, (__bf16)f); }
__device__ __forceinline__ unsigned pk2bf(float lo, float hi) { bf16x2 v = {(__bf16)lo, (__bf16)hi}; return __builtin_bit_cast(unsigned, v); }
__device__ __forceinline__ float bf2f(unsigned b) { return __builtin_bit_cast(float, b << 16); }
__device__ __forceinline__ float rbf(float f) { return bf2f(f2bf(f)); }
__device__ __forceinline__ float asf(unsigned u) { return __builtin_bit_cast(float, u); }
__device__ __forceinline__ unsigned asu(float f) { return __builtin_bit_cast(unsigned, f); }
template <int CTRL> __device__ __forceinline__ float dpp(float x) {
  return asf((unsigned)__builtin_amdgcn_mov_dpp((int)asu(x), CTRL, 0xf, 0xf, true));
}
template <int CTRL, int RM, int BM> __device__ __forceinline__ unsigned dppu(unsigned old, unsigned x) {
  return (unsigned)__builtin_amdgcn_update_dpp((int)old, (int)x, CTRL, RM, BM, false);
}
constexpr int XOR1 = 0xB1, XOR2 = 0x4E, XOR7 = 0x141, XOR8 = 0x128, ROR4 = 0x124, NEWB = 0x150;
__device__ __forceinline__ float tr16(float a, float b) {
  auto s = __builtin_amdgcn_permlane16_swap(asu(a), asu(b), false, false);
  return asf(s[0]) + asf(s[1]);
}
__device__ __forceinline__ float tr32(float a, float b) {
  auto s = __builtin_amdgcn_permlane32_swap(asu(a), asu(b), false, false);
  return asf(s[0]) + asf(s[1]);
}
__device__ __forceinline__ float max_rows(float x) {
  auto s = __builtin_amdgcn_permlane16_swap(asu(x), asu(x), false, false);
  x = fmaxf(asf(s[0]), asf(s[1]));
  auto t = __builtin_amdgcn_permlane32_swap(asu(x), asu(x), false, false);
  return fmaxf(asf(t[0]), asf(t[1]));
}
__device__ __forceinline__ float sum8(float x) { x += dpp<XOR1>(x); x += dpp<XOR2>(x); x += dpp<XOR7>(x); return x; }
__device__ __forceinline__ float max8(float x) { x = fmaxf(x, dpp<XOR1>(x)); x = fmaxf(x, dpp<XOR2>(x)); return fmaxf(x, dpp<XOR7>(x)); }
__device__ __forceinline__ float wave_sum(float x) { x = sum8(x); x += dpp<XOR8>(x); x = tr16(x, x); return tr32(x, x); }
__device__ __forceinline__ float wave_max(float x) { x = max8(x); x = fmaxf(x, dpp<XOR8>(x)); return max_rows(x); }
__device__ __forceinline__ float fexp(float x) { return __builtin_amdgcn_exp2f(x * 1.4426950408889634f); }
__device__ __forceinline__ v4f mfma(v4u a, v4u b, v4f c) {
  return __builtin_amdgcn_mfma_f32_16x16x32_bf16(__builtin_bit_cast(bf16x8, a), __builtin_bit_cast(bf16x8, b), c, 0, 0, 0);
}
__device__ __forceinline__ unsigned umax3(unsigned a, unsigned b, unsigned c) { return max(max(a, b), c); }
__device__ __forceinline__ unsigned pack_w(float v, unsigned t) { return ((asu(v) + 0x8u) & 0xfffffff0u) | t; }
__device__ __forceinline__ float unpack_w(unsigned w) { return asf(w & 0xfffffff0u); }
__device__ __forceinline__ unsigned chunk_tag(unsigned tag, int c) {
  const unsigned t = (tag >> (16 * (c & 1))) & 0xffffu, x = (t | (t << 8)) & 0x00ff00ffu;
  return (x | (x << 4)) & 0x0f0f0f0fu;
}
__device__ __forceinline__ v4u pack4(float a, float b, float c, float d, unsigned ct) {
  return v4u{pack_w(a, ct & 0xfu), pack_w(b, (ct >> 8) & 0xfu), pack_w(c, (ct >> 16) & 0xfu), pack_w(d, ct >> 24)};
}
__device__ __forceinline__ u64 bad_lanes(v4u v, unsigned ct) {
  const unsigned lo = __builtin_amdgcn_perm(v[1], v[0], 0x0c0c0400u), hi = __builtin_amdgcn_perm(v[3], v[2], 0x04000c0cu);
  return __builtin_amdgcn_ballot_w64(((lo | hi) & 0x0f0f0f0fu) != ct);
}
__device__ __forceinline__ v4u pack2k(unsigned a, unsigned b, unsigned tag) { return v4u{a | (tag & 0xfu), b | ((tag >> 4) & 0xfu), tag, ~tag}; }
__device__ __forceinline__ u64 bad_lanes2k(v4u v, unsigned tag) {
  return __builtin_amdgcn_ballot_w64(((((v[0] ^ tag) | (v[1] ^ (tag >> 4))) & 0xfu) | (v[2] ^ tag) | (v[3] ^ ~tag)) != 0u);
}
__device__ __forceinline__ u64 widen_g8(u64 m) { m |= m >> 4; m |= m >> 2; m |= m >> 1; m &= 0x0101010101010101ull; return (m << 8) - m; }
__device__ __forceinline__ u64 widen_k8(u64 m) { m |= m >> 32; m |= m >> 16; m |= m >> 8; m &= 0xffull; m |= m << 8; m |= m << 16; return m | (m << 32); }
__device__ __forceinline__ float red4x8(float a, float b, float c, float d) {
  float x = tr16(tr32(a, c), tr32(b, d));
  return x + dpp<XOR8>(x);
}
__device__ __forceinline__ void axpy8(float (&acc)[8], v4u w, unsigned slo, unsigned shi) {
#pragma unroll
  for (int k = 0; k < 4; ++k) { acc[2 * k] = dot2(w[k], slo, acc[2 * k]); acc[2 * k + 1] = dot2(w[k], shi, acc[2 * k + 1]); }
}
__device__ __forceinline__ void reload1(v4u& a, u64 ma, int off, v4u rs) {
  asm volatile("s_mov_b64 exec, %[ma]\n\tbuffer_load_dwordx4 %[a], %[off], %[rs], 0 offen sc1\n\ts_mov_b64 exec, -1\n\ts_waitcnt vmcnt(0)"
               : [a] "+v"(a) : [ma] "s"(ma), [off] "v"(off), [rs] "s"(rs) : "memory");
}
template <int STRIDE> __device__ __forceinline__ void reload2(v4u& a, v4u& b, u64 ma, u64 mb, int off, v4u rs) {
  asm volatile("s_mov_b64 exec, %[ma]\n\tbuffer_load_dwordx4 %[a], %[off], %[rs], 0 offen sc1\n\t"
               "s_mov_b64 exec, %[mb]\n\tbuffer_load_dwordx4 %[b], %[off], %[rs], 0 offen offset:%[st] sc1\n\t"
               "s_mov_b64 exec, -1\n\ts_waitcnt vmcnt(0)"
               : [a] "+v"(a), [b] "+v"(b) : [ma] "s"(ma), [mb] "s"(mb), [off] "v"(off), [rs] "s"(rs), [st] "i"(STRIDE) : "memory");
}
__device__ __forceinline__ void reload3(v4u (&f)[3], const u64 (&m)[3], int off, v4u rs) {
  asm volatile("s_mov_b64 exec, %[m0]\n\tbuffer_load_dwordx4 %[a], %[off], %[rs], 0 offen sc1\n\t"
               "s_mov_b64 exec, %[m1]\n\tbuffer_load_dwordx4 %[b], %[off], %[rs], 0 offen offset:1024 sc1\n\t"
               "s_mov_b64 exec, %[m2]\n\tbuffer_load_dwordx4 %[c], %[off], %[rs], 0 offen offset:2048 sc1\n\t"
               "s_mov_b64 exec, -1\n\ts_waitcnt vmcnt(0)"
               : [a] "+v"(f[0]), [b] "+v"(f[1]), [c] "+v"(f[2]) : [m0] "s"(m[0]), [m1] "s"(m[1]), [m2] "s"(m[2]), [off] "v"(off), [rs] "s"(rs) : "memory");
}
__device__ __forceinline__ v4u sel_a(unsigned vv, const unsigned (&msk)[4]) { return v4u{vv & msk[0], vv & msk[1], vv & msk[2], vv & msk[3]}; }

#define SB() __builtin_amdgcn_sched_barrier(0)
#define PACE() do { SB(); asm volatile("s_waitcnt vmcnt(5)" ::: "memory"); SB(); } while (0)
#define PIN(x) asm volatile("" : "+v"(x))
#define MEMBAR() asm volatile("" ::: "memory")
#define LD_G(off, so) __builtin_amdgcn_raw_buffer_load_b128(rws, off, so, AUX_SC1)
#define ST_G4(v, off) __builtin_amdgcn_raw_buffer_store_b128(v, rws, off, 0, AUX_SC1)
#define ST_G1(v, off) __builtin_amdgcn_raw_buffer_store_b32(v, rws, off, 0, AUX_SC1)
#define LDS_INC(off) do { MEMBAR(); if (lane == 0) __hip_atomic_fetch_add((LAS unsigned*)(L + L_SY + (off)), 1u, __ATOMIC_RELAXED, __HIP_MEMORY_SCOPE_WORKGROUP); MEMBAR(); } while (0)
#define LDS_WAIT(off, n) do { _Pragma("unroll 1") for (unsigned sp_ = 0; sp_ < LSPIN_CAP; ++sp_) { \
      if (__builtin_amdgcn_readfirstlane(__hip_atomic_load((LAS unsigned*)(L + L_SY + (off)), __ATOMIC_RELAXED, __HIP_MEMORY_SCOPE_WORKGROUP)) >= (n)) break; \
      __builtin_amdgcn_s_sleep(1); } MEMBAR(); } while (0)
#define STEP(n, LOAD) do { v2u g_; _Pragma("unroll 1") for (unsigned sp_ = 0; sp_ < LSPIN_CAP; ++sp_) { MEMBAR(); g_ = *(const LAS v2u*)(L + L_SY + SY_G); \
      if (__builtin_amdgcn_readfirstlane(g_[0]) >= 8 * ((n) - 1)) break; __builtin_amdgcn_s_sleep(1); } \
    { const unsigned t0_ = __builtin_amdgcn_readfirstlane(g_[1]); \
      if ((int)((unsigned)__builtin_amdgcn_s_memrealtime() - t0_) < TOFF2 + (n) * TAU2) { SB(); asm volatile("s_waitcnt vmcnt(0)" ::: "memory"); SB(); } \
      _Pragma("unroll 1") for (int i_ = 0; i_ < 200 && (int)((unsigned)__builtin_amdgcn_s_memrealtime() - t0_) < TOFF + (n) * TAU; ++i_) __builtin_amdgcn_s_sleep(1); } \
    SB(); LOAD; LDS_INC(SY_G); SB(); } while (0)
#define BAR() do { SB(); asm volatile("s_waitcnt lgkmcnt(0)" ::: "memory"); __builtin_amdgcn_s_barrier(); MEMBAR(); SB(); } while (0)
#define GET_TAG() ({ unsigned e_; asm volatile("s_load_dword %0, %1, 0x0\n\ts_waitcnt lgkmcnt(0)" : "=s"(e_) : "s"(ws) : "memory"); \
    const unsigned i_ = (((e_ ^ (e_ >> 15)) * 0x9e3779b1u + 0x7f4a7c15u) >> 3) * 0x85ebca6bu | 0x11111111u; \
    ((e_ & 0x77777777u) + (i_ & 0x77777777u)) ^ ((e_ ^ i_) & 0x88888888u); })

__device__ __forceinline__ int h1_off(int hd, int jj, int line, int al, int pol) {
  const int idx = ((hd & 3) * 8 + jj) * 768 + line * 128;
  return al + WS_H1 + ((hd >> 2) & 3) * 49152 + (idx >> 13) * 16384 + (pol ^ (hd >> 4)) * 8192 + (idx & 8191);
}
__device__ __forceinline__ int hs_off(int slot, int al, int pol) {
  const int q = (slot >> 3) * 4 + (slot & 3);
  return al + WS_HS + (q >> 1) * 16384 + (pol ^ ((slot >> 2) & 1)) * 8192 + (q & 1) * 4096;
}
#define GET_POL() ({ LDS_WAIT(SY_POL, 1); (int)__builtin_amdgcn_readfirstlane(*(const LAS unsigned*)(L + L_SY + SY_POL)) - 1; })
__global__ void __launch_bounds__(768, 3)
fused_decode(const u16* __restrict__ x, const u16* __restrict__ Kc, const u16* __restrict__ Vc, const u16* __restrict__ wq,
             const u16* __restrict__ wg, const u16* __restrict__ wu, unsigned char* ws, const u16* __restrict__ wo, const u16* __restrict__ wd,
             u16* __restrict__ out) {
  __shared__ __attribute__((aligned(16))) char smem[LDS_BYTES];
  LAS char* const L = (LAS char*)smem;
  const int lane = threadIdx.x & 63, wv = __builtin_amdgcn_readfirstlane(threadIdx.x >> 6);
  const int b = blockIdx.x, g8 = b & 7, ii = b >> 3, h = g8 * 4 + (ii & 3), j = ii >> 2;
  const bool tile = ii < 30;
  const int rb = tile ? ii / 10 : 0, cbk = tile ? ii - 10 * rb : 0;
  const int k = lane & 7, g = lane >> 3, row = lane >> 4;
  int kvoff = (wv < 4 ? OOB : 0) + g * ROWB + k * 16;
  const int sw = (wv - 4) & 7;
  PIN(kvoff);
  asm volatile("" :: "s"(x), "s"(Kc), "s"(Vc), "s"(wq), "s"(wg), "s"(wu), "s"(ws), "s"(wo));
  const __amdgpu_buffer_rsrc_t rws = __builtin_amdgcn_make_buffer_rsrc((void*)ws, 0, WS_END, 0x00020000);
  const v4u rsw = {(unsigned)(uint64_t)ws, (unsigned)((uint64_t)ws >> 32) & 0xffffu, (unsigned)WS_END, 0x00020000u};
  const int al = (int)(-(unsigned)(uint64_t)ws & 16383u);
  const size_t kvbase = ((size_t)h * SK + (SK - WIN)) * DH;
  const __amdgpu_buffer_rsrc_t rk = __builtin_amdgcn_make_buffer_rsrc((void*)(Kc + kvbase), 0, WIN * ROWB, 0x00020000);
  v4u kr[8][3];
#define LDK(a) do { _Pragma("unroll") for (int r_ = 0; r_ < 3; ++r_) kr[a][r_] = __builtin_amdgcn_raw_buffer_load_b128(rk, kvoff + r_ * 128, ((((a) + g8) & 7) * 64 + sw * 8 + ((j + (a) + sw) & 7)) * 8 * ROWB, AUX_NT); SB(); } while (0)
#define LDV(a) do { _Pragma("unroll") for (int r_ = 0; r_ < 3; ++r_) vr[a][r_] = __builtin_amdgcn_raw_buffer_load_b128(rv, kvoff + r_ * 128, ((((a) + g8) & 7) * 64 + sw * 8 + ((j + (a) + sw) & 7)) * 8 * ROWB, AUX_NT); SB(); } while (0)
  LDK(0);
  if (wv >= 4) {
    const int w = wv - 4;
    const __amdgpu_buffer_rsrc_t rv = __builtin_amdgcn_make_buffer_rsrc((void*)(Vc + kvbase), 0, WIN * ROWB, 0x00020000);
    v4u vr[8][3];
    BAR();
    LDK(1);
    LDS_INC(SY_G);
    PACE(); STEP(2, LDK(2));
    PACE(); STEP(3, LDK(3));
    PACE(); STEP(4, LDK(4));
    PACE(); STEP(5, LDK(5));
    PACE(); STEP(6, LDK(6));
    PACE(); STEP(7, LDK(7));
    PACE(); STEP(8, LDV(0));
    PACE(); STEP(9, LDV(1)); LDV(2); LDS_INC(SY_G);
    const unsigned tag = GET_TAG();
    LDS_WAIT(SY_Q, 3);
    v4u qf[3];
#pragma unroll
    for (int r = 0; r < 3; ++r) qf[r] = *(const LAS v4u*)(L + L_Q + r * 128 + k * 16);
    float sc[8];
#pragma unroll
    for (int a = 0; a < 8; ++a) {
      float d[3];
#pragma unroll
      for (int r = 0; r < 3; ++r) { d[r] = 0.f;
#pragma unroll
        for (int e = 0; e < 4; ++e) d[r] = dot2(kr[a][r][e], qf[r][e], d[r]); }
      sc[a] = (d[0] + d[1]) + d[2];
    }
#pragma unroll
    for (int i = 0; i < 4; ++i) { const float t0 = sc[i] + dpp<XOR7>(sc[i]), t1 = sc[i + 4] + dpp<XOR7>(sc[i + 4]); sc[i] = (k & 4) ? t1 : t0; }
#pragma unroll
    for (int i = 0; i < 2; ++i) { const float t0 = sc[i] + dpp<XOR2>(sc[i]), t1 = sc[i + 2] + dpp<XOR2>(sc[i + 2]); sc[i] = (k & 2) ? t1 : t0; }
    float s;
    { const float t0 = sc[0] + dpp<XOR1>(sc[0]), t1 = sc[1] + dpp<XOR1>(sc[1]); s = (k & 1) ? t1 : t0; }
    s = rbf(rbf(s) * 0.07216878364870322f);
    const float mw = wave_max(s);
    const float pf = fexp(s - mw);
    const float lw = wave_sum(pf);
    if (lane == 0) *(LAS v2f*)(L + L_ML + w * 8) = v2f{mw, lw};
    const unsigned pp = pk2bf(pf, pf);
    unsigned msk[4];
#pragma unroll
    for (int e = 0; e < 4; ++e) msk[e] = (k >> 1) == e ? ((k & 1) ? 0xffff0000u : 0x0000ffffu) : 0u;
    v4f oa[3];
#pragma unroll
    for (int r = 0; r < 3; ++r) oa[r] = v4f{0.f, 0.f, 0.f, 0.f};
#define PV(a) do { unsigned p_ = dppu<NEWB + (a), 0xf, 0x3>(0u, pp); p_ = dppu<NEWB + 8 + (a), 0xf, 0xC>(p_, pp); \
    const v4u a_ = sel_a(p_, msk); \
    _Pragma("unroll") for (int r_ = 0; r_ < 3; ++r_) oa[r_] = mfma(a_, vr[a][r_], oa[r_]); SB(); } while (0)
    const int ln = 8 * rb + w, hh = g8 * 4 + ln / 6, lh = ln % 6;
    const __amdgpu_buffer_rsrc_t ro = __builtin_amdgcn_make_buffer_rsrc((void*)(wo + ((size_t)g8 * 768 + ln * 32) * D + cbk * 64), 0,
                                                                        !tile ? 0 : 31 * 1280 + 128, 0x00020000);
    const int wooff = (8 * (g & 3) + (g >> 2)) * 1280 + k * 16;
    v4u wr[4];
#define LDWO(lo, hi) do { _Pragma("unroll") for (int e_ = (lo); e_ < (hi); ++e_) wr[e_] = __builtin_amdgcn_raw_buffer_load_b128(ro, wooff, e_ * 2560, AUX_NT); SB(); } while (0)
    PV(0);
    PACE(); STEP(11, LDV(3)); PV(1);
    PACE(); STEP(12, LDV(4)); PV(2);
    PACE(); STEP(13, LDV(5)); PV(3);
    PACE(); STEP(14, LDV(6)); PV(4);
    int m_off = tile ? WS_HM + hh * 128 + k * 16 : OOB;
    v4u pm;
    PACE(); STEP(15, { pm = LD_G(m_off, 0); LDV(7); });
    LDS_WAIT(SY_G, 120);
    LDWO(0, 2);
    PV(5);
    __builtin_amdgcn_s_setprio(2);
    unsigned ow_addr = L_O + (2 * w + (lane >> 5)) * 800 + k * 32 + (row & 1) * 16;
    unsigned or_addr = L_O + row * 4 * 800 + (32 * w + 2 * (lane & 15)) * 4;
    unsigned sw_addr = L_S + (2 * w + (lane >> 5)) * 272 + k * 32 + (row & 1) * 16;
    const int pol = GET_POL() & 1;
    int st_off = lane < 32 ? h1_off(h, j, w, al, pol) + lane * 4 : OOB;
    int o_off = h1_off(hh, k, lh, al, pol) + g * 16;
    unsigned tagb = (tag >> (4 * k)) & 0xfu;
    unsigned ctg = chunk_tag(tag, g);
    const bool valid = (g & 1) == (lane >> 5);
    PIN(ctg); PIN(ow_addr); PIN(or_addr); PIN(sw_addr); PIN(st_off); PIN(o_off); PIN(tagb);
    unsigned mt;
#pragma unroll 1
    for (unsigned sp = 0; sp < LSPIN_CAP; ++sp) { MEMBAR(); mt = *(const LAS unsigned*)(L + L_ML + k * 8); if (__builtin_amdgcn_ballot_w64(mt == SENT) == 0ull) break; }
    float cw = fexp(mw - max8(asf(mt)));
    PIN(cw);
    SB();
    PV(6);
    LDWO(2, 4);
    float cj;
#define WEIGHTS() do { const float mj_ = unpack_w(pm[0]); cj = fexp(mj_ - max8(mj_)); cj /= sum8(cj * unpack_w(pm[1])); PIN(cj); } while (0)
    u64 mm = bad_lanes2k(pm, tag);
    WEIGHTS();
    SB();
    PV(7);
    if (valid) {
#pragma unroll
      for (int r = 0; r < 3; ++r) *(LAS v4f*)(L + ow_addr + r * 256) = oa[r] * cw;
    }
    MEMBAR();
    if (w < 6) {
      v2u t0, t1, t2, t3;
#pragma unroll 1
      for (unsigned sp = 0; sp < LSPIN_CAP; ++sp) {
        MEMBAR();
        t0 = *(const LAS v2u*)(L + or_addr); t1 = *(const LAS v2u*)(L + or_addr + 800); t2 = *(const LAS v2u*)(L + or_addr + 1600); t3 = *(const LAS v2u*)(L + or_addr + 2400);
        if (__builtin_amdgcn_ballot_w64(max(umax3(t0[0], t1[0], t2[0]), t3[0]) == SENT) == 0ull) break;
      }
      float own = tr16((asf(t0[0]) + asf(t1[0])) + (asf(t2[0]) + asf(t3[0])), (asf(t0[1]) + asf(t1[1])) + (asf(t2[1]) + asf(t3[1])));
      own = tr32(own, own);
      ST_G1(pack_w(own, tagb), st_off);
      SB();
    } else {
#pragma unroll 1
      for (unsigned sp = 0; sp < LSPIN_CAP; ++sp) { MEMBAR(); if (__builtin_amdgcn_ballot_w64(*(const LAS unsigned*)(L + L_O + k * 1600) == SENT) == 0ull) break; }
      __builtin_amdgcn_s_sleep(2);
    }
    if (tile) {
      if (mm != 0ull) {
#pragma unroll 1
        for (unsigned spin = 0; spin < SPIN_CAP; ++spin) {
          reload1(pm, mm, m_off, rsw);
          mm = bad_lanes2k(pm, tag);
          if (mm == 0ull) break;
          __builtin_amdgcn_s_sleep(2);
        }
        WEIGHTS();
      }
      __builtin_amdgcn_s_sleep(DLY1);
      v4u po = {0u, 0u, 0u, 0u};
      u64 m0 = ~0ull;
#pragma unroll 1
      for (unsigned spin = 0; spin < SPIN_CAP; ++spin) {
        reload1(po, m0, o_off, rsw);
        m0 = bad_lanes(po, ctg);
        if (m0 == 0ull) break;
        m0 = widen_k8(m0);
      }
      v4f sa = {0.f, 0.f, 0.f, 0.f}, sb = sa;
      float o[4];
#pragma unroll
      for (int e = 0; e < 4; ++e) o[e] = sum8(cj * unpack_w(po[e]));
      sa = mfma(sel_a(pk2bf(o[0], o[0]), msk), wr[0], sa); sb = mfma(sel_a(pk2bf(o[1], o[1]), msk), wr[1], sb);
      sa = mfma(sel_a(pk2bf(o[2], o[2]), msk), wr[2], sa); sb = mfma(sel_a(pk2bf(o[3], o[3]), msk), wr[3], sb);
      if (valid) *(LAS v4f*)(L + sw_addr) = sa + sb;
    }
  } else {
    const int u = wv, tid = threadIdx.x;
    const unsigned t_own = (unsigned)__builtin_amdgcn_s_memrealtime();
    const int hp = ii, jp = g8;
    const int row0 = 80 * jp + 20 * u;
    const v4u xa = *(const GAS v4u*)(x + lane * 8);
    const v4u xb = *(const GAS v4u*)(x + 512 + (lane & 15) * 8);
    const unsigned xr = x[row0 + (lane < 20 ? lane : 19)];
    const int hi4 = hp & 3, hstart = (hp >> 2) * 192 + (hi4 == 0 ? 0 : 32 + 32 * hi4), hchunks = (hi4 == 0 || hi4 == 3) ? 8 : 4;
    v4u w1[20];
    {
      const int c = lane < 39 ? lane : 39, cf = min((c - 24) & 7, hchunks - 1);
      uint64_t pa = (uint64_t)(wq + (size_t)row0 * (H * DH) + hp * DH), pb = (uint64_t)(wg + (size_t)row0 * F + hstart),
               pc = (uint64_t)(wu + (size_t)row0 * F + hstart);
      asm volatile("" : "+s"(pa), "+s"(pb), "+s"(pc));
      const u16* p = (const u16*)(c < 24 ? pa : (c < 32 ? pb : pc)) + (c < 24 ? c : cf) * 8;
      const int stride = c < 24 ? H * DH : F;
#pragma unroll
      for (int i = 0; i < 20; ++i) w1[i] = __builtin_nontemporal_load((const GAS v4u*)(p + (size_t)i * stride));
    }
    SB();
    if (tid < 8) *(LAS unsigned*)(L + L_SY + tid * 4) = tid * 4 == SY_T0 ? t_own : 0u;
#pragma unroll
    for (int i = 0; i < 5; ++i) *(LAS v4u*)(L + L_O + i * 4096 + tid * 16) = v4u{SENT, SENT, SENT, SENT};
    BAR();
    const unsigned tag = GET_TAG();
    float xs[16];
#pragma unroll
    for (int i = 0; i < 4; ++i) { xs[2 * i] = asf(xa[i] << 16); xs[2 * i + 1] = asf(xa[i] & 0xffff0000u); xs[8 + 2 * i] = asf(xb[i] << 16); xs[8 + 2 * i + 1] = asf(xb[i] & 0xffff0000u); }
    const bool has_b = lane < 16;
    float s1 = 0.f, s1b = 0.f;
#pragma unroll
    for (int i = 0; i < 8; ++i) { s1 += xs[i]; s1b += xs[8 + i]; }
    const float mean = wave_sum(s1 + (has_b ? s1b : 0.f)) * (1.0f / D);
    float s2 = 0.f, s2b = 0.f;
#pragma unroll
    for (int i = 0; i < 8; ++i) { const float d0 = xs[i] - mean, d1 = xs[8 + i] - mean; s2 = fmaf(d0, d0, s2); s2b = fmaf(d1, d1, s2b); }
    const float var = wave_sum(s2 + (has_b ? s2b : 0.f)) * (1.0f / D);
    const float den = rbf(__builtin_sqrtf(rbf(rbf(var) + 1e-5f)));
    const unsigned xn = f2bf(rbf(bf2f(xr) - rbf(mean)) / den);
    asm volatile("" :: "v"(kr[0][0]), "v"(kr[0][1]), "v"(kr[0][2]));
    {
      float acc[8];
#pragma unroll
      for (int i = 0; i < 8; ++i) acc[i] = 0.f;
#pragma unroll
      for (int i = 0; i < 20; ++i) { const unsigned s = __builtin_amdgcn_readlane(xn, i); axpy8(acc, w1[i], s, s << 16); }
      *(LAS v4f*)(L + L_P0 + tid * 32) = v4f{acc[0], acc[1], acc[2], acc[3]};
      *(LAS v4f*)(L + L_P0 + tid * 32 + 16) = v4f{acc[4], acc[5], acc[6], acc[7]};
    }
    LDS_INC(SY_P0);
    if (u == 3 && b < 8) ST_G4(pack2k(t_own << 4, 0u, tag), lane == 0 ? WS_HT + b * 16 : OOB);
    if (u == 3 && b == 240) {
      int dt = 0;
#pragma unroll 1
      for (int r = 0; r < 4; ++r) {
        const unsigned char* p = ws + al + WS_PR + (r & 1) * 8192 + (r >> 1) * 512;
        unsigned long long t0, t1; unsigned d0, d1, d2, d3;
        asm volatile("s_memtime %0\n\ts_load_dword %2, %6, 0x0\n\ts_load_dword %3, %6, 0x900\n\ts_load_dword %4, %6, 0x1000\n\ts_load_dword %5, %6, 0x1900\n\t"
                     "s_waitcnt lgkmcnt(0)\n\ts_memtime %1\n\ts_waitcnt lgkmcnt(0)"
                     : "=&s"(t0), "=&s"(t1), "=&s"(d0), "=&s"(d1), "=&s"(d2), "=&s"(d3) : "s"(p) : "memory");
        const int e = (int)(t1 - t0);
        dt += (r & 1) ? -e : e;
      }
      const unsigned pol = dt > 0 ? 1u : 0u;
      ST_G4(pack2k(pol << 4, 0u, tag), lane == 0 ? WS_HP : OOB);
    }
    if (u < 2) {
      LDS_WAIT(SY_P0, 4);
      if (tid < 48 + 4 * hchunks) {
        const int c = tid - 48;
        const int a0 = tid < 48 ? tid * 16 : (192 + 2 * c) * 4, a1 = tid < 48 ? tid * 16 + 8 : (256 + 2 * c) * 4;
        v2f x0 = {0.f, 0.f}, x1 = {0.f, 0.f};
#pragma unroll
        for (int ww = 0; ww < 4; ++ww) { x0 += *(LAS v2f*)(L + L_P0 + ww * 2048 + a0); x1 += *(LAS v2f*)(L + L_P0 + ww * 2048 + a1); }
        ST_G4(pack4(x0[0], x0[1], x1[0], x1[1], chunk_tag(tag, tid)),
              tid < 48 ? WS_H0 + (hp * 8 + jp) * 768 + tid * 16 : WS_HG + ((hstart >> 4) + (c >> 3)) * 1024 + jp * 128 + (c & 7) * 16);
      }
    }
    const __amdgpu_buffer_rsrc_t rd = __builtin_amdgcn_make_buffer_rsrc((void*)(wd + ((size_t)g8 * 192 + rb * 64 + u * 16) * D + cbk * 64), 0,
                                                                        !tile ? 0 : 15 * 1280 + 128, 0x00020000);
    const v4u wd0 = __builtin_amdgcn_raw_buffer_load_b128(rd, g * 2560 + k * 16, 0, AUX_NT), wd1 = __builtin_amdgcn_raw_buffer_load_b128(rd, g * 2560 + k * 16, 1280, AUX_NT);
    asm volatile("s_waitcnt vmcnt(0)" ::: "memory");
    if (u != 0) __builtin_amdgcn_s_sleep(16);
    const int g_off = tile ? WS_HG + (g8 * 12 + rb * 4 + u) * 1024 + k * 128 + g * 16 : OOB;
    v4u pg = LD_G(g_off, 0);
    const unsigned ctk = chunk_tag(tag, k);
    if (u < 3) {
      const int q_off = WS_H0 + (h * 8 + g) * 768 + k * 16 + u * 256;
      v4u pq0 = {0u, 0u, 0u, 0u}, pq1 = pq0;
      u64 mq0 = ~0ull, mq1 = ~0ull;
#pragma unroll 1
      for (unsigned spin = 0; spin < SPIN_CAP; ++spin) {
        reload2<128>(pq0, pq1, mq0, mq1, q_off, rsw);
        mq0 = bad_lanes(pq0, ctk); mq1 = bad_lanes(pq1, ctk);
        if ((mq0 | mq1) == 0ull) break;
        mq0 = widen_g8(mq0); mq1 = widen_g8(mq1);
        __builtin_amdgcn_s_sleep(4);
      }
      const float q0 = red4x8(unpack_w(pq0[0]), unpack_w(pq0[1]), unpack_w(pq0[2]), unpack_w(pq0[3]));
      const float q1 = red4x8(unpack_w(pq1[0]), unpack_w(pq1[1]), unpack_w(pq1[2]), unpack_w(pq1[3]));
      *(LAS u16*)(L + L_Q + (u * 64 + (g & 1) * 32 + 4 * k + 2 * (lane >> 5) + (row & 1)) * 2) = (u16)f2bf((lane & 8) ? q1 : q0);
      LDS_INC(SY_Q);
    }
    if (u == 3) {
      const v4u t = LD_G(WS_HT + k * 16, 0);
      const u64 bad = bad_lanes2k(t, tag);
      int d = ((bad >> lane) & 1) ? 0 : (int)((t[0] & 0xfffffff0u) - (t_own << 4)) >> 4;
      d = max(min(d, 0), -300);
      d = min(d, __builtin_amdgcn_mov_dpp(d, XOR1, 0xf, 0xf, true)); d = min(d, __builtin_amdgcn_mov_dpp(d, XOR2, 0xf, 0xf, true)); d = min(d, __builtin_amdgcn_mov_dpp(d, XOR7, 0xf, 0xf, true));
      if (lane == 0) *(LAS unsigned*)(L + L_SY + SY_T0) = t_own + (unsigned)d;
    }
    unsigned tagb = (tag >> (4 * k)) & 0xfu;
    v4u ha[2];
    if (tile) {
#pragma unroll 1
      for (unsigned spin = 0; spin < SPIN_CAP; ++spin) {
        const u64 m0 = bad_lanes(pg, chunk_tag(tag, g));
        if (m0 == 0ull) break;
        __builtin_amdgcn_s_sleep(4);
        reload1(pg, widen_k8(m0), g_off, rsw);
      }
      unsigned msk[4];
#pragma unroll
      for (int e = 0; e < 4; ++e) msk[e] = (k >> 1) == e ? ((k & 1) ? 0xffff0000u : 0x0000ffffu) : 0u;
#pragma unroll
      for (int e = 0; e < 2; ++e) {
        const float ga = rbf(sum8(unpack_w(pg[e]))), up = rbf(sum8(unpack_w(pg[2 + e])));
        const float inner = 0.7978845608028654f * (ga + 0.044715f * ga * ga * ga);
        const float hd = rbf(ga / (1.0f + fexp(-2.0f * inner))) * up;
        ha[e] = sel_a(pk2bf(hd, hd), msk);
        PIN(ha[e]);
      }
    }
    float dsum;
    if (tile) {
      v4f da = mfma(ha[0], wd0, v4f{0.f, 0.f, 0.f, 0.f});
      da = mfma(ha[1], wd1, da);
      if ((g & 1) == (lane >> 5)) *(LAS v4f*)(L + L_D + (2 * u + (lane >> 5)) * 272 + k * 32 + (row & 1) * 16) = da;
      unsigned t0, t1;
#pragma unroll 1
      for (unsigned sp = 0; sp < LSPIN_CAP; ++sp) {
        MEMBAR();
        t0 = *(const LAS unsigned*)(L + L_D + row * 544 + (u * 16 + (lane & 15)) * 4); t1 = *(const LAS unsigned*)(L + L_D + row * 544 + 272 + (u * 16 + (lane & 15)) * 4);
        if (__builtin_amdgcn_ballot_w64(max(t0, t1) == SENT) == 0ull) break;
        __builtin_amdgcn_s_sleep(1);
      }
      dsum = asf(t0) + asf(t1);
      dsum = tr16(dsum, dsum); dsum = tr32(dsum, dsum); PIN(dsum);
    }
    if (u == 2) {
      LDS_WAIT(SY_G, POLAT);
      v4u t = {0u, 0u, 0u, 0u};
#pragma unroll 1
      for (unsigned spin = 0; spin < SPIN_CAP; ++spin) {
        reload1(t, 1ull, WS_HP, rsw);
        if ((bad_lanes2k(t, tag) & 1ull) == 0ull) break;
        __builtin_amdgcn_s_sleep(8);
      }
      if (lane == 0) *(LAS unsigned*)(L + L_SY + SY_POL) = 1u + ((t[0] >> 4) & 1u);
    }
    if (u == 3) {
      v2u t;
#pragma unroll 1
      for (unsigned sp = 0; sp < LSPIN_CAP; ++sp) { MEMBAR(); t = *(const LAS v2u*)(L + L_ML + k * 8); if (__builtin_amdgcn_ballot_w64(t[0] == SENT) == 0ull) break; __builtin_amdgcn_s_sleep(2); }
      const float m = max8(asf(t[0])), l = sum8(fexp(asf(t[0]) - m) * asf(t[1]));
      ST_G4(pack2k(pack_w(m, 0u), pack_w(l, 0u), tag), lane == 0 ? WS_HM + h * 128 + j * 16 : OOB);
    }
    const int pol = GET_POL() & 1;
    if (tile) {
      if (u == 0) {
        LDS_WAIT(SY_G, PRET);
        const int ln = 8 * rb + g;
        const unsigned t = __builtin_amdgcn_raw_buffer_load_b32(rws, h1_off(g8 * 4 + ln / 6, k, ln % 6, al, pol), 0, AUX_SC1);
        asm volatile("" :: "v"(t));
      }
      unsigned sr_addr = L_S + row * 4 * 272 + (u * 16 + (lane & 15)) * 4;
      int st_off = lane < 32 ? hs_off(cbk * 4 + u, al, pol) + (g8 * 3 + rb) * 128 + lane * 4 : OOB;
      PIN(sr_addr); PIN(st_off); PIN(tagb);
      dsum *= 0.5f;
      PIN(dsum);
      LDS_WAIT(SY_G, 120);
      unsigned t0, t1, t2, t3;
#pragma unroll 1
      for (unsigned sp = 0; sp < LSPIN_CAP; ++sp) {
        MEMBAR();
        t0 = *(const LAS unsigned*)(L + sr_addr); t1 = *(const LAS unsigned*)(L + sr_addr + 272); t2 = *(const LAS unsigned*)(L + sr_addr + 544); t3 = *(const LAS unsigned*)(L + sr_addr + 816);
        if (__builtin_amdgcn_ballot_w64(max(umax3(t0, t1, t2), t3) == SENT) == 0ull) break;
      }
      float ss = (asf(t0) + asf(t1)) + (asf(t2) + asf(t3));
      ss = tr32(ss, ss); ss = tr16(ss, dsum);
      ST_G1(pack_w(ss, tagb), st_off);
    } else {
      const int slot = (ii - 30) * 8 + g8 + 16 * u;
      if (slot < 40) {
        const int col = 16 * slot + 4 * (lane & 3) + 2 * (lane >> 5) + (row & 1);
        const __amdgpu_buffer_rsrc_t rout = __builtin_amdgcn_make_buffer_rsrc((void*)out, 0, D * 2, 0x00020000);
        int out_off = lane < 4 ? col * 2 : OOB, f_off = hs_off(slot, al, pol) + lane * 16;
        float xv = bf2f(x[col]);
        LDS_WAIT(SY_G, PRET);
        const unsigned t = __builtin_amdgcn_raw_buffer_load_b32(rws, lane < 24 ? hs_off(slot, al, pol) + lane * 128 : OOB, 0, AUX_SC1);
        asm volatile("" :: "v"(t));
        PIN(xv); PIN(out_off); PIN(f_off);
#pragma unroll 1
        for (unsigned sp = 0; sp < LSPIN_CAP; ++sp) {
          MEMBAR();
          if (__builtin_amdgcn_ballot_w64(*(const LAS unsigned*)(L + L_O + k * 1600) == SENT) == 0ull) break;
          __builtin_amdgcn_s_sleep(1);
        }
        __builtin_amdgcn_s_sleep(DLY2);
        v4u f[3]; u64 m[3];
#pragma unroll
        for (int i = 0; i < 3; ++i) { f[i] = v4u{0u, 0u, 0u, 0u}; m[i] = ~0ull; }
#pragma unroll 1
        for (unsigned spin = 0; spin < SPIN_CAP; ++spin) {
          reload3(f, m, f_off, rsw);
#pragma unroll
          for (int i = 0; i < 3; ++i) m[i] = bad_lanes(f[i], ctk);
          if ((m[0] | m[1] | m[2]) == 0ull) break;
#pragma unroll
          for (int i = 0; i < 3; ++i) m[i] = widen_g8(m[i]);
        }
        float c4[4];
#pragma unroll
        for (int e = 0; e < 4; ++e) c4[e] = (unpack_w(f[0][e]) + unpack_w(f[1][e])) + unpack_w(f[2][e]);
        const float v = rbf(red4x8(c4[0], c4[1], c4[2], c4[3]));
        const float gt = rbf(v / (1.0f + fexp(-v)));
        const unsigned ob = f2bf(xv + rbf(v * dpp<ROR4>(gt)));
        auto p16 = __builtin_amdgcn_permlane16_swap(ob, ob, false, false);
        const unsigned pr = p16[0] | (p16[1] << 16);
        auto p32 = __builtin_amdgcn_permlane32_swap(pr, pr, false, false);
        __builtin_amdgcn_raw_buffer_store_b64(v2u{p32[0], p32[1]}, rout, out_off, 0, AUX_SC1 | 1);
        if (slot == 0 && lane == 0) __builtin_amdgcn_raw_buffer_store_b32(tag, rws, WS_EPOCH, 0, AUX_SC1);
      }
    }
  }
}

extern "C" void kernel_launch(void* const* d_in, const int* in_sizes, int n_in, void* d_out, int out_size, void* d_ws, size_t ws_size,
                              hipStream_t stream) {
  fused_decode<<<256, 768, 0, stream>>>((const u16*)d_in[0], (const u16*)d_in[1], (const u16*)d_in[2], (const u16*)d_in[3],
                                        (const u16*)d_in[6], (const u16*)d_in[7], (unsigned char*)d_ws, (const u16*)d_in[4], (const u16*)d_in[8], (u16*)d_out);
}
```
